```python
import torch

def _softmax(x, axis=-1):
    return torch.softmax(x, dim=axis)
def _gelu(x):
    return torch.nn.functional.gelu(x, approximate="tanh")
def _silu(x):
    return torch.nn.functional.silu(x)
def _sigmoid(x):
    return torch.sigmoid(x)
def _layernorm(x, axis=-1, eps=1e-5):
    m = x.mean(dim=axis, keepdim=True)
    v = x.var(dim=axis, keepdim=True, unbiased=False)
    return (x - m) / torch.sqrt(v + eps)
def _scatter_add(tgt, idx, src, axis=0):
    return tgt.clone().index_add_(axis, idx, src)
def _segment_sum(src, seg, n):
    out = torch.zeros((n,) + src.shape[1:], dtype=src.dtype)
    return out.index_add_(0, seg, src)
def _pad(x, pw):
    flat = tuple(v for pair in reversed(pw) for v in pair)
    return torch.nn.functional.pad(x, flat)

kernel_name = 'standalone__grouped__vision'

def setup_inputs():

    _w = (lambda a: (lambda t: t.to(torch.bfloat16) if t.is_floating_point() else t)(torch.as_tensor(a)))
    H = W = 384
    G = 4
    Cg = max(96 // G, 2)
    C = G * Cg
    k = 3
    scale = 1.0 / (k * Cg ** 0.5)
    return {
        "x": _w((lambda *s: torch.randn(*s, dtype=torch.bfloat16) * 0.05)(1, H, W, C)),
        "w": _w((lambda *s: torch.randn(*s, dtype=torch.bfloat16) * 0.05)(k, k, G, Cg, Cg) * scale),
    }

def reference(x, w):
    B, H, W, C = x.shape
    k = w.shape[0]
    G = w.shape[2]
    Cg = C // G
    pad = k // 2
    xp = _pad(x, ((0,0),(pad,pad),(pad,pad),(0,0)))
    xg = xp.reshape(B, H + 2 * pad, W + 2 * pad, G, Cg)
    out = torch.zeros_like(x).reshape(B, H, W, G, Cg)
    for i in range(k):
        for j in range(k):
            out = out + torch.einsum('bhwgc,gcd->bhwgd', xg[:, i:i+H, j:j+W, :, :], w[i, j])
    return out.reshape(B, H, W, C)
```

```cpp
#include <hip/hip_runtime.h>
#include <hip/hip_bf16.h>
constexpr int H=384,W=384,C=96,G=4,CG=24;
__global__ void naive_k(const __hip_bfloat16* x,const __hip_bfloat16* w,__hip_bfloat16* o){
  int idx=blockIdx.x*blockDim.x+threadIdx.x;
  if(idx>=H*W*C) return;
  int c=idx%C, p=idx/C, xx=p%W, y=p/W; int g=c/CG,d=c%CG;
  float acc=0;
  for(int i=0;i<3;i++)for(int j=0;j<3;j++){
    int yy=y+i-1,xc=xx+j-1; if(yy<0||yy>=H||xc<0||xc>=W) continue;
    const __hip_bfloat16* xp=x+((size_t)yy*W+xc)*C+g*CG;
    const __hip_bfloat16* wp=w+(((i*3+j)*G+g)*CG)*CG+d;
    for(int k=0;k<CG;k++) acc+=__bfloat162float(xp[k])*__bfloat162float(wp[k*CG]);
  }
  o[idx]=__float2bfloat16(acc);
}
extern "C" void kernel_launch(void* const* d_in,const int* in_sizes,int n_in,void* d_out,int out_size,void* d_ws,size_t ws_size,hipStream_t stream){
  naive_k<<<(H*W*C+255)/256,256,0,stream>>>((const __hip_bfloat16*)d_in[0],(const __hip_bfloat16*)d_in[1],(__hip_bfloat16*)d_out);
}
```

```cpp
#define AUX_ST 16
#define STORE_START 2
#include <hip/hip_runtime.h>
#include <hip/hip_bf16.h>
#include <stdint.h>

#define LDPOL ""
#define PRIO 1

typedef __attribute__((ext_vector_type(8))) __bf16 bf16x8;
typedef __attribute__((ext_vector_type(2))) __bf16 bf16x2;
typedef __attribute__((ext_vector_type(4))) short s16x4;
typedef __attribute__((ext_vector_type(4))) float f32x4;
typedef __attribute__((ext_vector_type(2))) float f32x2;
typedef __attribute__((ext_vector_type(4))) unsigned u32x4;
typedef __attribute__((ext_vector_type(2))) unsigned u32x2;
#define LDS __attribute__((address_space(3)))

constexpr int IMG = 384;
constexpr int PIXB = 192;
constexpr int ROWB = IMG * PIXB;
constexpr int XBYTES = IMG * ROWB;
constexpr int WBYTES = 9 * 4 * 24 * 24 * 2;
constexpr int TH = 12, TW = 48;
constexpr int LROW = (TW + 2) * PIXB;
constexpr int OROW = TW * PIXB;
constexpr int ST0 = 14 * LROW;
constexpr int WSLOT = 11 * 1024;
constexpr int WB = 163840 - 4 * WSLOT;
constexpr unsigned OOB = 0x80000000u;
constexpr int XBIAS = 256;

__device__ __forceinline__ unsigned pk(float a, float b) {
  f32x2 v = {a, b};
  return __builtin_bit_cast(unsigned, __builtin_convertvector(v, bf16x2));
}
__device__ __forceinline__ f32x4 mm(bf16x8 a, bf16x8 b, f32x4 c) {
  return __builtin_amdgcn_mfma_f32_16x16x32_bf16(a, b, c, 0, 0, 0);
}
__device__ __forceinline__ bf16x8 trpair(LDS char* s, int a) {
  s16x4 lo = __builtin_amdgcn_ds_read_tr16_b64_v4i16((LDS s16x4*)(s + a));
  s16x4 hi = __builtin_amdgcn_ds_read_tr16_b64_v4i16((LDS s16x4*)(s + a + 192));
  return __builtin_bit_cast(bf16x8, __builtin_shufflevector(lo, hi, 0, 1, 2, 3, 4, 5, 6, 7));
}
__device__ __forceinline__ int swz(int c, int f) { return (c & 12) | ((c ^ f) & 3); }
__device__ __forceinline__ constexpr int stage_base(int st) { return st == 0 ? ST0 : (2 * st - 2) * LROW; }

#define DMA1(v, off) "buffer_load_dwordx4 " v ", %5, %6 offen offset:" #off " " LDPOL " lds\n\t"
#define DMA2(v, off) "buffer_load_dwordx4 " v ", %5, %7 offen offset:" #off " " LDPOL " lds\n\t"
#define DMA3(v, off) "buffer_load_dwordx4 " v ", %5, %8 offen offset:" #off " " LDPOL " lds\n\t"
__device__ __forceinline__ void dma_row(unsigned vf, unsigned v0, unsigned v1, unsigned v2, unsigned vl, __amdgpu_buffer_rsrc_t r, int soff, int lds) {
  asm volatile("s_mov_b32 m0, %9\n\ts_nop 0\n\t"
               DMA1("%0", 0) DMA1("%2", 1024) DMA1("%3", 2048) DMA1("%1", 3072)
               "s_add_u32 m0, m0, 0x1000\n\ts_nop 0\n\t"
               DMA2("%2", 0) DMA2("%3", 1024) DMA2("%1", 2048) DMA2("%2", 3072)
               "s_add_u32 m0, m0, 0x1000\n\ts_mov_b64 exec, 0xffffff\n\t"
               DMA3("%4", 1024)
               "s_mov_b64 exec, -1\n\t"
               DMA3("%3", 0)
               ::"v"(vf), "v"(v0), "v"(v1), "v"(v2), "v"(vl), "s"(r), "s"(soff), "s"(soff + 4096), "s"(soff + 8192), "s"(lds) : "memory", "scc");
}
__device__ __forceinline__ void dma_w(__amdgpu_buffer_rsrc_t r, int lds, unsigned voff) {
  asm volatile("s_mov_b32 m0, %0\n\ts_nop 0\n\tbuffer_load_dwordx4 %1, %2, 0 offen lds" ::"s"(lds), "v"(voff), "s"(r) : "memory");
}
#define WAITVM(n) asm volatile("s_waitcnt vmcnt(" #n ")" ::: "memory")
#define RAWBAR() do { asm volatile("" ::: "memory"); __builtin_amdgcn_s_barrier(); asm volatile("" ::: "memory"); } while (0)
#define BAR() do { asm volatile("s_waitcnt lgkmcnt(0)" ::: "memory"); __builtin_amdgcn_s_barrier(); asm volatile("" ::: "memory"); } while (0)
#define SB() __builtin_amdgcn_sched_barrier(0)
#define PIN(x) asm volatile("" : "+v"(x))
#define WAITLGKM0() __builtin_amdgcn_s_waitcnt(0xC07F)
constexpr bool NOEPI = false;
constexpr bool NORD = false;

#define STAMP(k)
#define RSTAMP(k)
#define LSTAMP(k)

__global__ __launch_bounds__(512) void gconv_k(const char* __restrict__ x, const char* __restrict__ w, char* __restrict__ out
) {
  __shared__ __attribute__((aligned(1024))) char smem[163840];
  LDS char* s = (LDS char*)smem;
  const int sbase = (int)(size_t)s;
  const int tid = threadIdx.x, lane = tid & 63;
  const int wv = __builtin_amdgcn_readfirstlane(tid >> 6);
  const int b = blockIdx.x, xcd = b & 7, idx = b >> 3;
  const int ty = (xcd >> 1) * 8 + (idx >> 2), tx = (xcd & 1) * 4 + (idx & 3);
  const int flip = ty & 1, y0 = ty * TH, x0 = tx * TW;
#define ECHO(k)

  auto finish = [&]() {
  };
  if (wv < 4) {
    if (PRIO) __builtin_amdgcn_s_setprio(PRIO);
    const int g = wv;
    {
      const __amdgpu_buffer_rsrc_t rw = __builtin_amdgcn_make_buffer_rsrc((void*)w, 0, WBYTES, 0x00020000);
#pragma unroll
      for (int k = 0; k < 11; k++) {
        const int i = 64 * k + lane, tap = (i * 911) >> 16;
        dma_w(rw, sbase + WB + g * WSLOT + k * 1024, i < 648 ? (unsigned)(16 * i + 3456 * tap + 1152 * g) : OOB);
      }
    }
    const int kg = lane >> 4, n = lane & 15;
    const int lz = g < 2 ? 0 : 2;
    const int j0 = (kg & 1) * 2, c0 = kg >> 1;
    const int j1 = kg < 2 ? (kg & 1) * 2 : 1;
    const int c1 = kg < 2 ? 2 : (g < 2 ? kg - 1 : kg - 2);
    const int pi = n < 4 ? 2 * n : (n < 12 ? 2 * n - 7 : 2 * n - 16);
    int aP0 = (pi + j0) * PIXB + swz(3 * g + c0, (pi + j0) >> 2) * 16;
    int aP1 = (pi + j1) * PIXB + swz(3 * g + c1, (pi + j1) >> 2) * 16;
    int aL = kg * LROW + (pi + 1) * PIXB + swz(3 * g + lz, (pi + 1) >> 2) * 16;
    int aW0 = ST0 + pi * PIXB + swz(3 * g + (kg >> 1), pi >> 1) * 16 + (kg & 1) * 8;
    int aW1 = ST0 + (kg >> 1) * OROW + pi * PIXB + swz(3 * g + 2, pi >> 1) * 16 + (kg & 1) * 8;
    int wa[17];
    {
      const int q = n >> 2, pp = n & 3;
      const int wg = WB + g * WSLOT + q * 48, ZB = WB + g * WSLOT + 9 * 1152;
      const int b0 = wg + j0 * 1152 + c0 * 384, b1 = wg + j1 * 1152 + c1 * 384, bl = wg + 1152 + lz * 384;
      auto trow = [&](int i) { return (flip ? 2 - i : i) * 3456; };
      const int dy = pp >> 1, dd = 32 + (pp & 1) * 8;
#pragma unroll
      for (int i = 0; i < 3; i++) { wa[2 * i] = b0 + trow(i) + pp * 8; wa[2 * i + 1] = b1 + trow(i) + pp * 8; }
#pragma unroll
      for (int r = 0; r < 4; r++) {
        const int i = r - dy;
        const bool ok = (unsigned)i <= 2u;
        wa[6 + 2 * r] = ok ? b0 + trow(i) + dd : ZB;
        wa[7 + 2 * r] = ok ? b1 + trow(i) + dd : ZB;
      }
      wa[14] = kg <= 2 ? bl + trow(kg) + pp * 8 : ZB;
      wa[15] = kg >= 1 ? bl + trow(kg - 1) + pp * 8 : ZB;
      const int il = kg - dy;
      wa[16] = (unsigned)il <= 2u ? bl + trow(il) + dd : ZB;
#pragma unroll
      for (int k = 0; k < 17; k++) PIN(wa[k]);
      PIN(aP0); PIN(aP1); PIN(aL); PIN(aW0); PIN(aW1);
    }
    LSTAMP(2);
    WAITVM(0);
    LSTAMP(3);
    bf16x8 AM[3][2], AT[4][2], AL0, AL2, ATL;
#pragma unroll
    for (int i = 0; i < 3; i++) { AM[i][0] = trpair(s, wa[2 * i]); AM[i][1] = trpair(s, wa[2 * i + 1]); }
#pragma unroll
    for (int r = 0; r < 4; r++) { AT[r][0] = trpair(s, wa[6 + 2 * r]); AT[r][1] = trpair(s, wa[7 + 2 * r]); }
    AL0 = trpair(s, wa[14]); AL2 = trpair(s, wa[15]); ATL = trpair(s, wa[16]);
    SB();
    LSTAMP(4);
    BAR();
    LSTAMP(5);
    ECHO(5);
    auto rdp = [&](int a, int r, int ct) { return *(const LDS bf16x8*)(s + a + (r % 6) * LROW + ct * 3072); };
    bf16x8 S0[3][2], S1[3][2], P2[2], P3[2], L;
    S0[0][0] = rdp(aP0, 0, 0); S1[0][0] = rdp(aP0, 1, 0); S0[0][1] = rdp(aP1, 0, 0); S1[0][1] = rdp(aP1, 1, 0);
    SB();
    P2[0] = rdp(aP0, 2, 0); P2[1] = rdp(aP1, 2, 0); L = rdp(aL, 0, 0); P3[0] = rdp(aP0, 3, 0); P3[1] = rdp(aP1, 3, 0);
    SB();
    const f32x4 Z = {0, 0, 0, 0};
    f32x4 E0 = Z, E1 = Z, E2 = Z;
#define MM(T, A, B) T = mm(A, B, T); SB()
#pragma unroll
    for (int u = 0; u < 18; u++) {
      const int st = u / 3, ct = u % 3;
      const int pst = (u + 17) / 3 - 6, pct = (u + 17) % 3;
      const bool ep = u > 0 && !NOEPI;
      f32x4 T0, T1, T2;
      u32x2 w0, w1, w2;
      T0 = mm(AM[0][0], S0[ct][0], Z); SB();
      if (u == 0) { S0[1][0] = rdp(aP0, 0, 1); SB(); }
      T1 = mm(AT[0][0], S0[ct][0], Z); SB();
      if (u == 0) { S1[1][0] = rdp(aP0, 1, 1); SB(); }
      T2 = mm(AM[0][0], S1[ct][0], Z); SB();
      if (u == 0) { S0[1][1] = rdp(aP1, 0, 1); SB(); }
      if (ep) { w0[0] = pk(E0[0], E0[1]); SB(); }
      MM(T0, AM[0][1], S0[ct][1]);
      if (u == 0) { S1[1][1] = rdp(aP1, 1, 1); SB(); }
      if (ep) { w0[1] = pk(E0[2], E0[3]); SB(); }
      MM(T1, AT[0][1], S0[ct][1]);
      if (u == 0) { S0[2][0] = rdp(aP0, 0, 2); SB(); }
      if (ep) { w2[0] = pk(E2[0], E2[1]); SB(); }
      MM(T0, AM[1][0], S1[ct][0]);
      if (u == 0) { S1[2][0] = rdp(aP0, 1, 2); SB(); }
      if (ep) { w2[1] = pk(E2[2], E2[3]); SB(); }
      MM(T1, AT[1][0], S1[ct][0]);
      if (u == 0) { S0[2][1] = rdp(aP1, 0, 2); SB(); }
      if (u > 0) { WAITLGKM0(); SB(); }
      MM(T2, AM[0][1], S1[ct][1]);
      if (u == 0) { S1[2][1] = rdp(aP1, 1, 2); SB(); }
      if (ep) { w1[0] = pk(E1[0], E1[1]); SB(); }
      MM(T0, AM[1][1], S1[ct][1]);
      if (ep) { w1[1] = pk(E1[2], E1[3]); SB(); }
      MM(T1, AT[1][1], S1[ct][1]);
      if (ep) { *(LDS u32x2*)(s + aW0 + pct * 3072) = w0; SB(); }
      MM(T2, AM[1][0], P2[0]);
      if (ep) { *(LDS u32x2*)(s + aW0 + pct * 3072 + OROW) = w2; SB(); }
      MM(T0, AM[2][0], P2[0]);
      if (ep) { *(LDS u32x2*)(s + aW1 + pct * 3072) = w1; SB(); }
      MM(T1, AT[2][0], P2[0]);
      if (ct == 0 && st > 0) {
        const int d = stage_base(st) - stage_base(st - 1);
        aW0 += d; PIN(aW0); SB();
      }
      MM(T2, AM[1][1], P2[1]);
      if (ct == 0 && st > 0) { const int d = stage_base(st) - stage_base(st - 1); aW1 += d; PIN(aW1); SB(); }
      MM(T0, AM[2][1], P2[1]);
      const int ns = (u + 1) / 3, nc = (u + 1) % 3;
      if (ct == 2) {
        BAR(); LSTAMP(6 + st); if (st == 0) { ECHO(6); } if (st == 4) { ECHO(10); }
        SB();
      }
      if (ct == 2 && (ns == 2 || ns == 5)) { aP0 += 6 * LROW; aP1 += 6 * LROW; PIN(aP0); PIN(aP1); SB(); }
      if (ct == 2 && ns == 3) { aL += 6 * LROW; PIN(aL); SB(); }
      bf16x8 N2[2], N3[2], NL;
      const bool rd = u < 17 && !NORD;
      if (NORD) { N2[0] = S0[ct][0]; N2[1] = S0[ct][1]; N3[0] = S1[ct][0]; N3[1] = S1[ct][1]; NL = L;
                  asm volatile("" : "+v"(N2[0]), "+v"(N2[1]), "+v"(N3[0]), "+v"(N3[1])); }
      if (rd) { N2[0] = rdp(aP0, 2 * ns + 2, nc); SB(); }
      MM(T1, AT[2][1], P2[1]);
      if (rd) { N2[1] = rdp(aP1, 2 * ns + 2, nc); SB(); }
      MM(T2, AL2, L);
      if (rd) { N3[0] = rdp(aP0, 2 * ns + 3, nc); SB(); }
      MM(T0, AL0, L);
      if (rd) { N3[1] = rdp(aP1, 2 * ns + 3, nc); SB(); }
      MM(T1, ATL, L);
      if (rd) { NL = rdp(aL, 2 * ns, nc); SB(); }
      MM(T2, AM[2][0], P3[0]);
      MM(T1, AT[3][0], P3[0]);
      MM(T2, AM[2][1], P3[1]);
      MM(T1, AT[3][1], P3[1]);
      S0[ct][0] = P2[0]; S0[ct][1] = P2[1]; S1[ct][0] = P3[0]; S1[ct][1] = P3[1];
      if (u < 17) { P2[0] = N2[0]; P2[1] = N2[1]; P3[0] = N3[0]; P3[1] = N3[1]; L = NL; }
      if (NOEPI) asm volatile("" :: "v"(T0), "v"(T1), "v"(T2));
      E0 = T0; E1 = T1; E2 = T2;
    }
    {
      const u32x2 w0 = {pk(E0[0], E0[1]), pk(E0[2], E0[3])}, w2 = {pk(E2[0], E2[1]), pk(E2[2], E2[3])}, w1 = {pk(E1[0], E1[1]), pk(E1[2], E1[3])};
      *(LDS u32x2*)(s + aW0 + 2 * 3072) = w0;
      *(LDS u32x2*)(s + aW0 + 2 * 3072 + OROW) = w2;
      *(LDS u32x2*)(s + aW1 + 2 * 3072) = w1;
    }
    BAR();
    LSTAMP(12);
    finish();
    return;
  }
  if (wv == 4) {
    unsigned v[3];
#pragma unroll
    for (int m = 0; m < 3; m++) {
      const int sl = 64 * m + lane, p = (sl * 171) >> 11;
      v[m] = lane * 16 + ((((sl ^ (p >> 2)) & 3) - (sl & 3)) << 4);
    }
    const unsigned vf = (tx == 0 && lane < 12) ? OOB : v[0];
    const unsigned vl = (tx == 7 && lane >= 12) ? OOB : v[0];
    const int xoff = (x0 - 1) * PIXB + XBIAS;
    const int ybase = flip ? y0 + 12 : y0 - 1;
    const int ystep = flip ? -1 : 1;
    LSTAMP(2);
#pragma unroll 1
    for (int r = 0; r < 14; r++) {
      if (r >= 6) WAITVM(50);
      if (r >= 6) LSTAMP(8 + r);
      if (r == 5) LSTAMP(8);
      if (r == 9) RAWBAR();
      const int y = ybase + r * ystep;
      const bool ok = (unsigned)y < (unsigned)IMG;
      const __amdgpu_buffer_rsrc_t rx = __builtin_amdgcn_make_buffer_rsrc((void*)(x - XBIAS), 0, ok ? XBYTES + XBIAS : 0, 0x00020000);
      dma_row(vf, v[0], v[1], v[2], vl, rx, (ok ? y * ROWB : 0) + xoff, sbase + r * LROW);
      if (r == 11 || r == 13) RAWBAR();
    }
    LSTAMP(3);
    WAITVM(40); RAWBAR();
    LSTAMP(4);
    WAITVM(20); RAWBAR();
    LSTAMP(5);
    WAITVM(0); LSTAMP(6); RAWBAR();
    RAWBAR();
    RAWBAR();
    LSTAMP(7);
    finish();
    return;
  }
  {
    const int h = wv - 5;
    const __amdgpu_buffer_rsrc_t ro = __builtin_amdgcn_make_buffer_rsrc((void*)out, 0, XBYTES, 0x00020000);
    const int sl = h * 64 + lane, col = sl / 12;
    const int ystep = flip ? -ROWB : ROWB;
    const unsigned goff = (flip ? y0 + 11 : y0) * ROWB + (x0 + col) * PIXB + swz(sl - col * 12, col >> 1) * 16;
    const int rd = sl * 16;
    RAWBAR();
    LSTAMP(4);
    int u = 0, ua = rd + ST0, uc = 0;
    unsigned ug = goff;
#pragma unroll 1
    for (int st = 0; st < 7; st++) {
      RAWBAR();
      if (st < STORE_START) continue;
      const int lim = st < 6 ? 3 * st + 2 : 18;
#pragma unroll 1
      for (; u < lim; u++) {
        const u32x4 a0 = *(const LDS u32x4*)(s + ua), a1 = *(const LDS u32x4*)(s + ua + OROW);
        __builtin_amdgcn_raw_buffer_store_b128(a0, ro, ug, 0, AUX_ST);
        __builtin_amdgcn_raw_buffer_store_b128(a1, ro, ug + ystep, 0, AUX_ST);
        if (uc < 2) { uc++; ua += 3072; ug += 3072; }
        else { uc = 0; ua = rd + 2 * (u / 3) * LROW; ug += 2 * ystep - 2 * 3072; }
      }
    }
    LSTAMP(11);
    finish();
  }
}

extern "C" void kernel_launch(void* const* d_in, const int* in_sizes, int n_in, void* d_out, int out_size,
                              void* d_ws, size_t ws_size, hipStream_t stream) {
  gconv_k<<<256, 512, 0, stream>>>((const char*)d_in[0], (const char*)d_in[1], (char*)d_out
  );
}
```
